```python
import torch

def _softmax(x, axis=-1):
    return torch.softmax(x, dim=axis)
def _gelu(x):
    return torch.nn.functional.gelu(x, approximate="tanh")
def _silu(x):
    return torch.nn.functional.silu(x)
def _sigmoid(x):
    return torch.sigmoid(x)
def _layernorm(x, axis=-1, eps=1e-5):
    m = x.mean(dim=axis, keepdim=True)
    v = x.var(dim=axis, keepdim=True, unbiased=False)
    return (x - m) / torch.sqrt(v + eps)
def _scatter_add(tgt, idx, src, axis=0):
    return tgt.clone().index_add_(axis, idx, src)
def _segment_sum(src, seg, n):
    out = torch.zeros((n,) + src.shape[1:], dtype=src.dtype)
    return out.index_add_(0, seg, src)
def _pad(x, pw):
    flat = tuple(v for pair in reversed(pw) for v in pair)
    return torch.nn.functional.pad(x, flat)

kernel_name = 'standalone__paged_block_table_kv_radixattention__decode'

def setup_inputs():

    _w = (lambda a: (lambda t: t.to(torch.bfloat16) if t.is_floating_point() else t)(torch.as_tensor(a)))
    B, H, Hkv, Dh = 2, 32, 16, 128
    Lk = 4096
    bs = 16
    nb = max(2, Lk // bs)
    P = max(1, nb // 2)
    n_phys = P + B * (nb - P)
    shared = torch.arange(P, dtype=torch.int64)
    priv = P + torch.arange(B * (nb - P), dtype=torch.int64).reshape(B, nb - P)
    bt = torch.cat([torch.tile(shared[None, :], (B, 1)), priv], dim=1)
    ctx = nb * bs - (torch.arange(1, B + 1, dtype=torch.int64) * 2654435761) % max(1, (nb * bs - P * bs) // 2)
    return {
        "q": _w((lambda *s: torch.randn(*s, dtype=torch.bfloat16) * 0.8)(B, H, Dh)),
        "k_cache": _w((lambda *s: torch.randn(*s, dtype=torch.bfloat16) * 0.8)(n_phys, bs, Hkv, Dh)),
        "v_cache": _w((lambda *s: torch.randn(*s, dtype=torch.bfloat16) * 0.05)(n_phys, bs, Hkv, Dh)),
        "block_table": _w(bt.to(torch.int64)),
        "ctx_lens": _w(torch.as_tensor(ctx).to(torch.int64)),
    }

def reference(q, k_cache, v_cache, block_table, ctx_lens):
    B, H, Dh = q.shape
    n_phys, bs, Hkv, _ = k_cache.shape
    nb = block_table.shape[1]
    Lk = nb * bs
    rep = H // Hkv
    k = k_cache[block_table].reshape(B, Lk, Hkv, Dh)
    v = v_cache[block_table].reshape(B, Lk, Hkv, Dh)
    k = (lambda x, n, axis: torch.repeat_interleave(x, n, dim=axis))(k, rep, axis=2)
    v = (lambda x, n, axis: torch.repeat_interleave(x, n, dim=axis))(v, rep, axis=2)
    scores = torch.einsum("bhd,bkhd->bhk", q, k) * (Dh ** -0.5)
    pos = torch.arange(Lk)
    valid = pos[None, :] < ctx_lens[:, None]
    scores = torch.where(valid[:, None, :], scores, -1e9)
    p = _softmax(scores, axis=-1)
    return torch.einsum("bhk,bkhd->bhd", p, v)
```

```cpp
#include <hip/hip_runtime.h>
#include <hip/hip_bf16.h>
#include <stdint.h>

typedef __bf16 bf16x8 __attribute__((ext_vector_type(8)));
typedef float f32x4 __attribute__((ext_vector_type(4)));
typedef unsigned u32x4 __attribute__((ext_vector_type(4)));
typedef int i32x4 __attribute__((ext_vector_type(4)));

constexpr int H = 32, HKV = 16, D = 128, PAGE = 16, NB = 256;
constexpr int NSPLIT = 16;
#define NWAVE 5
#define NCH 2
#define KV_AUX 2
constexpr int W = NWAVE;
constexpr float NEG = -1e30f;
constexpr float C2 = 0.088388347648318440f * 1.4426950408889634f;

__device__ __forceinline__ unsigned cvtpk(float lo, float hi) {
  typedef float f32x2_ __attribute__((ext_vector_type(2))); typedef __bf16 bf16x2_ __attribute__((ext_vector_type(2)));
  return __builtin_bit_cast(unsigned, __builtin_convertvector(f32x2_{lo, hi}, bf16x2_));
}
__device__ __forceinline__ float xrow_max(float x) {
  auto s = __builtin_amdgcn_permlane16_swap(__float_as_uint(x), __float_as_uint(x), false, false);
  x = fmaxf(__uint_as_float(s[0]), __uint_as_float(s[1]));
  auto t = __builtin_amdgcn_permlane32_swap(__float_as_uint(x), __float_as_uint(x), false, false);
  return fmaxf(__uint_as_float(t[0]), __uint_as_float(t[1]));
}
__device__ __forceinline__ float xrow_sum(float x) {
  auto s = __builtin_amdgcn_permlane16_swap(__float_as_uint(x), __float_as_uint(x), false, false);
  x = __uint_as_float(s[0]) + __uint_as_float(s[1]);
  auto t = __builtin_amdgcn_permlane32_swap(__float_as_uint(x), __float_as_uint(x), false, false);
  return __uint_as_float(t[0]) + __uint_as_float(t[1]);
}

__global__ void __launch_bounds__(W * 64)
attn_split(const unsigned short* __restrict__ q, const unsigned short* __restrict__ kc_, const unsigned short* __restrict__ vc_,
           const int* __restrict__ bt, const int* __restrict__ lens, float* __restrict__ po, float* __restrict__ pml, unsigned kv_bytes) {
  __shared__ __attribute__((aligned(16))) float lds[W * 4 * 128 + W * 8];
  const int tid = threadIdx.x, lane = tid & 63, wave = __builtin_amdgcn_readfirstlane(tid >> 6);
  const int g = lane >> 4, c = lane & 15;
  const int bid = blockIdx.x;
  const int xcd = bid & 7, jj = bid >> 3;
  const int s = 2 * xcd + (jj >> 4), kvh = jj & 15;

  const i32x4 bt0 = *reinterpret_cast<const i32x4*>(bt + 4 * lane);
  const i32x4 bt1 = *reinterpret_cast<const i32x4*>(bt + NB + 4 * lane);
  const int c0 = lens[0], c1 = lens[1];
  const int r = c >> 2;
  bf16x8 qf[4];
  {
    const unsigned short* qp = q + ((r >> 1) * H + kvh * 2 + (r & 1)) * D + g * 8;
#pragma unroll
    for (int k = 0; k < 4; ++k) qf[k] = *reinterpret_cast<const bf16x8*>(qp + k * 32);
  }
  const int len0 = c0 <= 0 ? NB * PAGE : min(c0, NB * PAGE), len1 = c1 <= 0 ? NB * PAGE : min(c1, NB * PAGE);
  const int np0 = (len0 + 15) >> 4, np1 = (len1 + 15) >> 4;
  const float cs = (r >> 1) ? (c1 <= 0 ? 0.f : C2) : (c0 <= 0 ? 0.f : C2);
  const int mylen = (r >> 1) ? len1 : len0;
  int mm = bt0[0] != bt1[0] ? 0 : bt0[1] != bt1[1] ? 1 : bt0[2] != bt1[2] ? 2 : bt0[3] != bt1[3] ? 3 : 4;
  const unsigned long long bal = __ballot(mm < 4);
  int ps = NB;
  if (bal) { const int fl = __builtin_ctzll(bal); ps = 4 * fl + __builtin_amdgcn_readlane(mm, fl); }
  ps = min(ps, min(np0, np1));
  const int U = np0 + np1 - ps;
  const int u0 = (s * U) >> 4, u1 = ((s + 1) * U) >> 4;

  const __amdgpu_buffer_rsrc_t krs = __builtin_amdgcn_make_buffer_rsrc((void*)kc_, 0, kv_bytes, 0x00020000);
  const __amdgpu_buffer_rsrc_t vrs = __builtin_amdgcn_make_buffer_rsrc((void*)vc_, 0, kv_bytes, 0x00020000);
  const unsigned kvo = c * 4096 + g * 16;
  const unsigned vvo = g * 4 * 4096 + c * 16;

  float m = NEG, l = 0.f;
  f32x4 acc[8];
#pragma unroll
  for (int i = 0; i < 8; ++i) acc[i] = f32x4{0.f, 0.f, 0.f, 0.f};

  for (int ub = u0; ub < u1; ub += 2 * W * NCH) {
    unsigned so[NCH][2]; int lim[NCH][2];
#pragma unroll
    for (int ch = 0; ch < NCH; ++ch)
#pragma unroll
      for (int t = 0; t < 2; ++t) {
        const int u = ub + (ch * W + wave) * 2 + t;
        const bool valid = u < u1;
        const bool isb1 = u >= np0;
        const int i = isb1 ? u - np0 + ps : u;
        const int ic = min(i, NB - 1);
        const int ln = ic >> 2, cp = ic & 3;
        const int a0 = isb1 ? __builtin_amdgcn_readlane(bt1[0], ln) : __builtin_amdgcn_readlane(bt0[0], ln);
        const int a1 = isb1 ? __builtin_amdgcn_readlane(bt1[1], ln) : __builtin_amdgcn_readlane(bt0[1], ln);
        const int a2 = isb1 ? __builtin_amdgcn_readlane(bt1[2], ln) : __builtin_amdgcn_readlane(bt0[2], ln);
        const int a3 = isb1 ? __builtin_amdgcn_readlane(bt1[3], ln) : __builtin_amdgcn_readlane(bt0[3], ln);
        const unsigned ph = cp == 0 ? a0 : cp == 1 ? a1 : cp == 2 ? a2 : a3;
        so[ch][t] = valid ? ph * 65536u + kvh * 256u : 0x80000000u;
        const bool mine = valid && ((r >> 1) ? (isb1 || u < ps) : !isb1);
        lim[ch][t] = mine ? mylen - 16 * i - 4 * g : 0;
      }
    u32x4 K[NCH][2][4], V[NCH][2][4];
#pragma unroll
    for (int ch = 0; ch < NCH; ++ch)
#pragma unroll
      for (int t = 0; t < 2; ++t)
#pragma unroll
        for (int k = 0; k < 4; ++k) K[ch][t][k] = __builtin_amdgcn_raw_buffer_load_b128(krs, kvo + k * 64, so[ch][t], KV_AUX);
#pragma unroll
    for (int ch = 0; ch < NCH; ++ch)
#pragma unroll
      for (int t = 0; t < 2; ++t)
#pragma unroll
        for (int k = 0; k < 4; ++k) V[ch][t][k] = __builtin_amdgcn_raw_buffer_load_b128(vrs, vvo, so[ch][t] + k * 4096u, KV_AUX);

    __builtin_amdgcn_sched_barrier(0);
    float sc[NCH][8];
    float mx = m;
#pragma unroll
    for (int ch = 0; ch < NCH; ++ch)
#pragma unroll
      for (int t = 0; t < 2; ++t) {
        f32x4 sacc = f32x4{0.f, 0.f, 0.f, 0.f};
#pragma unroll
        for (int k = 0; k < 4; ++k) sacc = __builtin_amdgcn_mfma_f32_16x16x32_bf16(__builtin_bit_cast(bf16x8, K[ch][t][k]), qf[k], sacc, 0, 0, 0);
#pragma unroll
        for (int j = 0; j < 4; ++j) { const float x = j < lim[ch][t] ? sacc[j] * cs : NEG; sc[ch][t * 4 + j] = x; mx = fmaxf(mx, x); }
      }
    mx = xrow_max(mx);
    const float alpha = __builtin_amdgcn_exp2f(m - mx);
    m = mx;
    l *= alpha;
    if (ub != u0) {
      const float ag = __shfl(alpha, 4 * g, 64);
#pragma unroll
      for (int i = 0; i < 8; ++i) acc[i][0] *= ag;
    }
#pragma unroll
    for (int ch = 0; ch < NCH; ++ch) {
      float p[8];
#pragma unroll
      for (int j = 0; j < 8; ++j) { p[j] = __builtin_amdgcn_exp2f(sc[ch][j] - mx); l += p[j]; }
      u32x4 pa = {cvtpk(p[0], p[1]), cvtpk(p[2], p[3]), cvtpk(p[4], p[5]), cvtpk(p[6], p[7])};
#pragma unroll
      for (int e = 0; e < 4; ++e) {
        u32x4 blo, bhi;
#pragma unroll
        for (int x = 0; x < 4; ++x) {
          const unsigned f = V[ch][x >> 1][(x & 1) * 2][e], sn = V[ch][x >> 1][(x & 1) * 2 + 1][e];
          blo[x] = __builtin_amdgcn_perm(sn, f, 0x05040100u);
          bhi[x] = __builtin_amdgcn_perm(sn, f, 0x07060302u);
        }
        acc[2 * e] = __builtin_amdgcn_mfma_f32_16x16x32_bf16(__builtin_bit_cast(bf16x8, pa), __builtin_bit_cast(bf16x8, blo), acc[2 * e], 0, 0, 0);
        acc[2 * e + 1] = __builtin_amdgcn_mfma_f32_16x16x32_bf16(__builtin_bit_cast(bf16x8, pa), __builtin_bit_cast(bf16x8, bhi), acc[2 * e + 1], 0, 0, 0);
      }
    }
  }
  l = xrow_sum(l);
  float* lo = lds + wave * 512 + g * 128 + c * 8;
  *reinterpret_cast<f32x4*>(lo) = f32x4{acc[0][0], acc[1][0], acc[2][0], acc[3][0]};
  *reinterpret_cast<f32x4*>(lo + 4) = f32x4{acc[4][0], acc[5][0], acc[6][0], acc[7][0]};
  float* lml = lds + W * 512;
  if (g == 0 && (c & 3) == 0) { lml[(r * W + wave) * 2] = m; lml[(r * W + wave) * 2 + 1] = l; }
  __syncthreads();
  if (tid < 128) {
    const int row = tid >> 5, d4 = (tid & 31) * 4;
    float mw[W], lw[W]; float M = NEG;
#pragma unroll
    for (int w = 0; w < W; ++w) { mw[w] = lml[(row * W + w) * 2]; lw[w] = lml[(row * W + w) * 2 + 1]; M = fmaxf(M, mw[w]); }
    f32x4 o = {0.f, 0.f, 0.f, 0.f}; float L = 0.f;
#pragma unroll
    for (int w = 0; w < W; ++w) {
      const float f = __builtin_amdgcn_exp2f(mw[w] - M);
      const f32x4 x = *reinterpret_cast<const f32x4*>(lds + w * 512 + row * 128 + d4);
      o += x * f; L = fmaf(lw[w], f, L);
    }
    const int slot = (kvh * 4 + row) * NSPLIT + s;
    *reinterpret_cast<f32x4*>(po + slot * 128 + d4) = o;
    if ((tid & 31) == 0) { pml[slot * 2] = M; pml[slot * 2 + 1] = L; }
  }
}

__global__ void __launch_bounds__(64) attn_combine(const float* __restrict__ po, const float* __restrict__ pml, unsigned short* __restrict__ out) {
  const int kr = blockIdx.x;
  const int kvh = kr >> 2, row = kr & 3, lane = threadIdx.x;
  float mw[NSPLIT], lw[NSPLIT]; float2 x[NSPLIT];
#pragma unroll
  for (int s = 0; s < NSPLIT; ++s) {
    const float2 ml = *reinterpret_cast<const float2*>(pml + (kr * NSPLIT + s) * 2); mw[s] = ml.x; lw[s] = ml.y;
    x[s] = *reinterpret_cast<const float2*>(po + (kr * NSPLIT + s) * 128 + 2 * lane);
  }
  float M = NEG;
#pragma unroll
  for (int s = 0; s < NSPLIT; ++s) M = fmaxf(M, mw[s]);
  float L = 0.f, o0 = 0.f, o1 = 0.f;
#pragma unroll
  for (int s = 0; s < NSPLIT; ++s) { const float f = __builtin_amdgcn_exp2f(mw[s] - M); L = fmaf(lw[s], f, L); o0 = fmaf(x[s].x, f, o0); o1 = fmaf(x[s].y, f, o1); }
  const float inv = 1.f / L;
  const unsigned pk = cvtpk(o0 * inv, o1 * inv);
  *reinterpret_cast<unsigned*>(out + ((row >> 1) * H + kvh * 2 + (row & 1)) * D + 2 * lane) = pk;
}

extern "C" void kernel_launch(void* const* d_in, const int* in_sizes, int n_in, void* d_out, int out_size, void* d_ws, size_t ws_size, hipStream_t stream) {
  const unsigned short* q = (const unsigned short*)d_in[0];
  const unsigned short* k = (const unsigned short*)d_in[1];
  const unsigned short* v = (const unsigned short*)d_in[2];
  const int* bt = (const int*)d_in[3];
  const int* lens = (const int*)d_in[4];
  float* po = (float*)d_ws;
  float* pml = po + HKV * 4 * NSPLIT * 128;
  const unsigned kv_bytes = (unsigned)in_sizes[1] * 2u;
  attn_split<<<256, W * 64, 0, stream>>>(q, k, v, bt, lens, po, pml, kv_bytes);
  attn_combine<<<HKV * 4, 64, 0, stream>>>(po, pml, (unsigned short*)d_out);
}
```

```cpp
#include <hip/hip_runtime.h>
#include <stdint.h>

typedef __bf16 bf16x8 __attribute__((ext_vector_type(8)));
typedef short s16x4 __attribute__((ext_vector_type(4)));
typedef float f32x4 __attribute__((ext_vector_type(4)));
typedef float f32x2 __attribute__((ext_vector_type(2)));
typedef __bf16 bf16x2 __attribute__((ext_vector_type(2)));
typedef unsigned u32x4 __attribute__((ext_vector_type(4)));
typedef unsigned u32x2 __attribute__((ext_vector_type(2)));
typedef int i32x4 __attribute__((ext_vector_type(4)));
typedef int i32x8 __attribute__((ext_vector_type(8)));
typedef unsigned u32x16 __attribute__((ext_vector_type(16)));
#define GAS __attribute__((address_space(1)))
#define CAS __attribute__((address_space(4)))

constexpr int H = 32, HKV = 16, D = 128, PAGE = 16, NB = 256;
constexpr int NSPLIT = 16;
#define NT 5
#define KV_AUX 2
constexpr int W = 4;
constexpr float NEG = -1e30f;
constexpr float C2 = 0.088388347648318440f * 1.4426950408889634f;
constexpr unsigned SENT = 0xFFFFFFFFu;
constexpr unsigned WS_E = 0, WS_R = 4096, WS_PO = 8192, WS_PML = WS_PO + 16 * 16 * 16 * 128, WS_END = WS_PML + 16 * 16 * 16 * 8;

struct Args { const void* q; const void* k; const void* v; const int* bt; const int* lens; void* out; void* ws; unsigned kv_bytes; unsigned pad; };

__device__ __forceinline__ unsigned cvtpk(float lo, float hi) {
  return __builtin_bit_cast(unsigned, __builtin_convertvector(f32x2{lo, hi}, bf16x2));
}
template <int CTRL> __device__ __forceinline__ float dpp(float x) {
  return __builtin_bit_cast(float, __builtin_amdgcn_mov_dpp(__builtin_bit_cast(int, x), CTRL, 0xf, 0xf, true));
}
__device__ __forceinline__ float xrow_max(float x) {
  auto s = __builtin_amdgcn_permlane16_swap(__float_as_uint(x), __float_as_uint(x), false, false);
  x = fmaxf(__uint_as_float(s[0]), __uint_as_float(s[1]));
  auto t = __builtin_amdgcn_permlane32_swap(__float_as_uint(x), __float_as_uint(x), false, false);
  return fmaxf(__uint_as_float(t[0]), __uint_as_float(t[1]));
}
__device__ __forceinline__ float xrow_sum(float x) {
  auto s = __builtin_amdgcn_permlane16_swap(__float_as_uint(x), __float_as_uint(x), false, false);
  x = __uint_as_float(s[0]) + __uint_as_float(s[1]);
  auto t = __builtin_amdgcn_permlane32_swap(__float_as_uint(x), __float_as_uint(x), false, false);
  return __uint_as_float(t[0]) + __uint_as_float(t[1]);
}
constexpr int ROR4 = 0x124, ROR8 = 0x128, ROR2 = 0x122, ROR1 = 0x121;
__device__ __forceinline__ float row_sum(float x) { x += dpp<ROR8>(x); x += dpp<ROR4>(x); x += dpp<ROR2>(x); x += dpp<ROR1>(x); return x; }
__device__ __forceinline__ float row_max(float x) { x = fmaxf(x, dpp<ROR8>(x)); x = fmaxf(x, dpp<ROR4>(x)); x = fmaxf(x, dpp<ROR2>(x)); x = fmaxf(x, dpp<ROR1>(x)); return x; }

#define STAMP(i)

__global__ void __launch_bounds__((W + 1) * 64) attn_fused(Args a_unused) {
  __shared__ __attribute__((aligned(16))) float lds[W * 512 + 32 + 516];
  const u32x16 ka = *(const CAS u32x16*)(__builtin_amdgcn_kernarg_segment_ptr());
  auto ptr64 = [&](int i) { return (unsigned long long)ka[2 * i] | ((unsigned long long)ka[2 * i + 1] << 32); };
  const int tid = threadIdx.x, lane = tid & 63, wave = __builtin_amdgcn_readfirstlane(tid >> 6);
  const int g = lane >> 4, c = lane & 15;
  const int bid = blockIdx.x;
  const int xcd = bid & 7, jj = bid >> 3;
  const int s = 2 * xcd + (jj >> 4), kvh = jj & 15;
  const __amdgpu_buffer_rsrc_t wrs = __builtin_amdgcn_make_buffer_rsrc((void*)ptr64(6), 0, WS_END, 0x00020000);
  unsigned long long tag = 0;

  if (wave < W) {
    const GAS int* bt = (const GAS int*)ptr64(3);
    const i32x4 bt0 = *reinterpret_cast<const GAS i32x4*>(bt + 4 * lane);
    const i32x4 bt1 = *reinterpret_cast<const GAS i32x4*>(bt + NB + 4 * lane);
    const CAS int* lens = (const CAS int*)ptr64(4);
    const int c0 = lens[0], c1 = lens[1];
    const int r = c >> 2;
    const bf16x8 qf = *reinterpret_cast<const GAS bf16x8*>((const GAS char*)ptr64(0) + (kvh * 2 * D * 2) + (unsigned)(((r >> 1) * H + (r & 1)) * D * 2 + g * 64 + (c & 3) * 16));
    __builtin_amdgcn_sched_barrier(0);
    int* tl = reinterpret_cast<int*>(lds + W * 512 + 32);
    tl[512] = 0x8000;
    const __amdgpu_buffer_rsrc_t krs = __builtin_amdgcn_make_buffer_rsrc((void*)ptr64(1), 0, ka[14], 0x00020000);
    const __amdgpu_buffer_rsrc_t vrs = __builtin_amdgcn_make_buffer_rsrc((void*)ptr64(2), 0, ka[14], 0x00020000);
    unsigned kvo = (c >> 2) * 4096 + g * 64 + (c & 3) * 16 + kvh * 256;
    unsigned vvo = g * 4096 + c * 16 + kvh * 256;
    float m = NEG, l = 0.f;
    f32x4 acc[8];
#pragma unroll
    for (int i = 0; i < 8; ++i) { acc[i] = f32x4{0.f, 0.f, 0.f, 0.f}; asm volatile("" : "+v"(acc[i])); }
    asm volatile("" : "+v"(kvo), "+v"(vvo), "+v"(m), "+v"(l));
    const unsigned lm0 = min((unsigned)(c0 - 1), (unsigned)(NB * PAGE - 1)), lm1 = min((unsigned)(c1 - 1), (unsigned)(NB * PAGE - 1));
    const int np0 = (int)(lm0 >> 4) + 1, np1 = (int)(lm1 >> 4) + 1;
    float cs = (r >> 1) ? (c1 <= 0 ? 0.f : C2) : (c0 <= 0 ? 0.f : C2);
    asm volatile("" : "+v"(cs));
    STAMP(1);
    __builtin_amdgcn_sched_barrier(0);
    unsigned ps = min((unsigned)np0, (unsigned)np1);
    *reinterpret_cast<i32x4*>(tl + 4 * lane) = bt0;
    *reinterpret_cast<i32x4*>(tl + NB + 4 * lane) = bt1;
#pragma unroll
    for (int k = 0; k < 4; ++k) {
      const unsigned long long ne = __builtin_amdgcn_ballot_w64(bt0[k] != bt1[k]);
      unsigned f; asm("s_ff1_i32_b64 %0, %1\n\ts_lshl2_add_u32 %0, %0, %2" : "=s"(f) : "s"(ne), "i"(k) : "scc");
      unsigned t2; asm("s_min_u32 %0, %1, %2" : "=s"(t2) : "s"(ps), "s"(f) : "scc");
      ps = t2;
    }
    const int U = np0 + np1 - (int)ps;
    const int u0 = (s * U) >> 4, u1 = ((s + 1) * U) >> 4;
    STAMP(2);
    int ub = u0;
#pragma clang loop unroll(disable)
    do {
      const int ui = ub + lane;
      const bool b1 = ui >= np0;
      const int li = b1 ? ui + ((int)ps - np0) : ui;
      const int idx = ui < u1 ? (b1 ? li + NB : li) : 512;
      const int ph = tl[idx];
      unsigned so[NT];
#pragma unroll
      for (int t = 0; t < NT; ++t) so[t] = (unsigned)__builtin_amdgcn_readlane(ph, t * W + wave) << 16;
      STAMP(3);
      u32x4 K[NT][4], V[NT][4];
#pragma unroll
      for (int t = 0; t < NT; ++t)
#pragma unroll
        for (int k = 0; k < 4; ++k) K[t][k] = __builtin_amdgcn_raw_buffer_load_b128(krs, kvo, so[t] + k * 16384u, KV_AUX);
#pragma unroll
      for (int t = 0; t < NT; ++t)
#pragma unroll
        for (int k = 0; k < 4; ++k) V[t][k] = __builtin_amdgcn_raw_buffer_load_b128(vrs, vvo, so[t] + k * 16384u, KV_AUX);
      __builtin_amdgcn_sched_barrier(0);
      STAMP(4);
      const int nv0 = (ui < u1 && !b1) ? (int)lm0 + 1 - 16 * li : 0;
      const int nv1 = (ui < u1 && (b1 || ui < (int)ps)) ? (int)lm1 + 1 - 16 * li : 0;
      int lim[NT];
#pragma unroll
      for (int t = 0; t < NT; ++t) { const int a0 = __builtin_amdgcn_readlane(nv0, t * W + wave), a1 = __builtin_amdgcn_readlane(nv1, t * W + wave); lim[t] = ((r >> 1) ? a1 : a0) - g; }
      float sc[NT][4];
      float mx = m;
#pragma unroll
      for (int t = 0; t < NT; ++t)
#pragma unroll
        for (int k = 0; k < 4; ++k) {
          const f32x4 d = __builtin_amdgcn_mfma_f32_16x16x32_bf16(__builtin_bit_cast(bf16x8, K[t][k]), qf, f32x4{0.f, 0.f, 0.f, 0.f}, 0, 0, 0);
          const float sum = dpp<0xFF>(d[3]) + (dpp<0xAA>(d[2]) + (dpp<0x55>(d[1]) + dpp<0x00>(d[0])));
          const float x = 4 * k < lim[t] ? sum * cs : NEG;
          sc[t][k] = x; mx = fmaxf(mx, x);
        }
      mx = xrow_max(mx);
      STAMP(5);
      {
        const float alpha = __builtin_amdgcn_exp2f(m - mx);
        l *= alpha;
        const float ag = __shfl(alpha, 4 * g, 64);
#pragma unroll
        for (int i = 0; i < 8; ++i) acc[i][0] *= ag;
      }
      m = mx;
      u32x2 pa[NT];
#pragma unroll
      for (int t = 0; t < NT; ++t) {
        float p[4];
#pragma unroll
        for (int j = 0; j < 4; ++j) { p[j] = __builtin_amdgcn_exp2f(sc[t][j] - mx); l += p[j]; }
        pa[t] = u32x2{cvtpk(p[0], p[1]), cvtpk(p[2], p[3])};
      }
      __builtin_amdgcn_sched_barrier(0);
#pragma unroll
      for (int t = 0; t < NT; ++t) {
#pragma unroll
        for (int e = 0; e < 4; ++e) {
          const u32x2 blo = {__builtin_amdgcn_perm(V[t][1][e], V[t][0][e], 0x05040100u), __builtin_amdgcn_perm(V[t][3][e], V[t][2][e], 0x05040100u)};
          const u32x2 bhi = {__builtin_amdgcn_perm(V[t][1][e], V[t][0][e], 0x07060302u), __builtin_amdgcn_perm(V[t][3][e], V[t][2][e], 0x07060302u)};
          acc[2 * e] = __builtin_amdgcn_mfma_f32_16x16x16bf16_1k(__builtin_bit_cast(s16x4, pa[t]), __builtin_bit_cast(s16x4, blo), acc[2 * e], 0, 0, 0);
          acc[2 * e + 1] = __builtin_amdgcn_mfma_f32_16x16x16bf16_1k(__builtin_bit_cast(s16x4, pa[t]), __builtin_bit_cast(s16x4, bhi), acc[2 * e + 1], 0, 0, 0);
        }
      }
      ub += W * NT;
    } while (ub < u1);
    l = xrow_sum(l);
    STAMP(6);
    float* lo = lds + wave * 512 + g * 128 + c * 8;
    *reinterpret_cast<f32x4*>(lo) = f32x4{acc[0][0], acc[1][0], acc[2][0], acc[3][0]};
    *reinterpret_cast<f32x4*>(lo + 4) = f32x4{acc[4][0], acc[5][0], acc[6][0], acc[7][0]};
    if (g == 0 && (c & 3) == 0) { lds[W * 512 + r * 4 + wave] = m; lds[W * 512 + 16 + r * 4 + wave] = l; }
  } else {
    GAS unsigned long long* ws64 = (GAS unsigned long long*)ptr64(6);
    const unsigned long long e0 = __hip_atomic_load(ws64 + (WS_E + kvh * 128) / 8, __ATOMIC_RELAXED, __HIP_MEMORY_SCOPE_AGENT);
    const u32x4 sent = {SENT, SENT, SENT, SENT};
    const unsigned o = WS_PO + ((kvh * 16 + (lane >> 2)) * 16 + s) * 128 + (lane & 3) * 32;
    __builtin_amdgcn_raw_buffer_store_b128(sent, wrs, o, 0, 16);
    __builtin_amdgcn_raw_buffer_store_b128(sent, wrs, o + 16, 0, 16);
    if (lane < 16) __builtin_amdgcn_raw_buffer_store_b64(u32x2{SENT, SENT}, wrs, WS_PML + ((kvh * 16 + lane) * 16 + s) * 8, 0, 16);
    tag = e0 + 1;
    asm volatile("s_waitcnt vmcnt(0)" ::: "memory");
    if (lane == 0) __hip_atomic_store(ws64 + (WS_R + kvh * 128 + s * 8) / 8, tag, __ATOMIC_RELAXED, __HIP_MEMORY_SCOPE_AGENT);
#pragma clang loop unroll(disable)
    for (unsigned spins = 0; spins < (1u << 20); ++spins) {
      const unsigned long long x = __hip_atomic_load(ws64 + (WS_R + kvh * 128 + c * 8) / 8, __ATOMIC_RELAXED, __HIP_MEMORY_SCOPE_AGENT);
      if (__all(x == tag)) break;
      __builtin_amdgcn_s_sleep(4);
    }
    if (s == 0 && lane == 0) __hip_atomic_store(ws64 + (WS_E + kvh * 128) / 8, tag, __ATOMIC_RELAXED, __HIP_MEMORY_SCOPE_AGENT);
    STAMP(1);
  }
  __syncthreads();
  STAMP(7);
  if (wave < 2) {
    const int row = tid >> 5, d4 = (tid & 31) * 4;
    const f32x4 mw = *reinterpret_cast<const f32x4*>(lds + W * 512 + row * 4);
    const f32x4 lw = *reinterpret_cast<const f32x4*>(lds + W * 512 + 16 + row * 4);
    const float M = fmaxf(fmaxf(mw[0], mw[1]), fmaxf(mw[2], mw[3]));
    f32x4 o = {0.f, 0.f, 0.f, 0.f}; float L = 0.f;
#pragma unroll
    for (int w = 0; w < W; ++w) {
      const float f = __builtin_amdgcn_exp2f(mw[w] - M);
      const f32x4 x = *reinterpret_cast<const f32x4*>(lds + w * 512 + row * 128 + d4);
      o += x * f; L = fmaf(lw[w], f, L);
    }
    const int cons = row * 4 + (d4 >> 5);
    u32x4 ob;
#pragma unroll
    for (int k = 0; k < 4; ++k) ob[k] = min(__float_as_uint(o[k]), SENT - 1u);
    __builtin_amdgcn_raw_buffer_store_b128(ob, wrs, WS_PO + ((kvh * 16 + cons) * 16 + s) * 128 + (d4 & 31) * 4, 0, 16);
    if ((tid & 7) == 0) __builtin_amdgcn_raw_buffer_store_b64(u32x2{min(__float_as_uint(M), SENT - 1u), min(__float_as_uint(L), SENT - 1u)}, wrs, WS_PML + ((kvh * 16 + cons) * 16 + s) * 8, 0, 16);
    STAMP(8);
  } else if (wave == W) {
    const unsigned o = WS_PO + ((kvh * 16 + s) * 16 + c) * 128 + g * 32;
    const unsigned oml = WS_PML + ((kvh * 16 + s) * 16 + c) * 8;
    u32x4 x0, x1; u32x2 ml;
#pragma clang loop unroll(disable)
    for (unsigned spins = 0; spins < (1u << 20); ++spins) {
      x0 = __builtin_amdgcn_raw_buffer_load_b128(wrs, o, 0, 16);
      x1 = __builtin_amdgcn_raw_buffer_load_b128(wrs, o + 16, 0, 16);
      ml = __builtin_amdgcn_raw_buffer_load_b64(wrs, oml, 0, 16);
      const bool ok = x0[0] != SENT && x0[1] != SENT && x0[2] != SENT && x0[3] != SENT && x1[0] != SENT && x1[1] != SENT && x1[2] != SENT && x1[3] != SENT && ml[0] != SENT && ml[1] != SENT;
      if (__all(ok)) break;
      asm volatile("" ::: "memory");
    }
    STAMP(8);
    const float mp = __uint_as_float(ml[0]);
    const float M = row_max(mp);
    const float f = __builtin_amdgcn_exp2f(mp - M);
    const float L = row_sum(__uint_as_float(ml[1]) * f);
    const float inv = 1.f / L;
    float o8[8];
#pragma unroll
    for (int k = 0; k < 4; ++k) { o8[k] = row_sum(__uint_as_float(x0[k]) * f) * inv; o8[4 + k] = row_sum(__uint_as_float(x1[k]) * f) * inv; }
    if (c == 0) {
      const int row = s >> 2;
      const u32x4 pk = {cvtpk(o8[0], o8[1]), cvtpk(o8[2], o8[3]), cvtpk(o8[4], o8[5]), cvtpk(o8[6], o8[7])};
      *reinterpret_cast<GAS u32x4*>((GAS char*)ptr64(5) + (((row >> 1) * H + kvh * 2 + (row & 1)) * D + (s & 3) * 32 + g * 8) * 2) = pk;
    }
    STAMP(9);
  }
}

extern "C" void kernel_launch(void* const* d_in, const int* in_sizes, int n_in, void* d_out, int out_size, void* d_ws, size_t ws_size, hipStream_t stream) {
  Args a;
  a.q = d_in[0]; a.k = d_in[1]; a.v = d_in[2]; a.bt = (const int*)d_in[3]; a.lens = (const int*)d_in[4];
  a.out = d_out; a.ws = d_ws; a.kv_bytes = (unsigned)in_sizes[1] * 2u; a.pad = 0;
  attn_fused<<<256, (W + 1) * 64, 0, stream>>>(a);
}
```

```cpp
#include <hip/hip_runtime.h>
#include <stdint.h>

typedef __bf16 bf16x8 __attribute__((ext_vector_type(8)));
typedef short s16x4 __attribute__((ext_vector_type(4)));
typedef float f32x4 __attribute__((ext_vector_type(4)));
typedef float f32x2 __attribute__((ext_vector_type(2)));
typedef __bf16 bf16x2 __attribute__((ext_vector_type(2)));
typedef unsigned u32x4 __attribute__((ext_vector_type(4)));
typedef unsigned u32x2 __attribute__((ext_vector_type(2)));
typedef int i32x4 __attribute__((ext_vector_type(4)));
typedef unsigned u32x16 __attribute__((ext_vector_type(16)));
#define GAS __attribute__((address_space(1)))
#define CAS __attribute__((address_space(4)))

constexpr int H = 32, D = 128, PAGE = 16, NB = 256;
constexpr int AUX_NT = 2, AUX_SC1 = 16;
constexpr unsigned OOB = 0x80000000u;
constexpr int NS = 19, NN = 15;
static_assert(NS + 3 * NN == 64 && NS - 16 == 3 && NN == 15, "consumer roles below");
constexpr int W = 4, NT = 5, NI = 4 * NT + 1;
constexpr int CONS_WAVE = 3;
constexpr float NEG = -1e30f;
constexpr float C2 = 0.088388347648318440f * 1.4426950408889634f;
constexpr unsigned WS_E = 0, WS_PO = 4096, WS_PML = WS_PO + 16 * 16 * 32 * 256, WS_END = WS_PML + 16 * 16 * 32 * 16;
constexpr int L_O = 0, L_M = W * 512, L_L = L_M + 16, L_TBL = L_L + 16, L_END = L_TBL + 516;

struct Map { unsigned short e[256]; };
struct Args { const void* q; const void* k; const void* v; const int* bt; const int* lens; void* out; void* ws; unsigned kv_bytes; unsigned pad; Map map; };

constexpr Map make_map() {
  int key[256] = {}, val[256] = {};
  int n = 0;
  for (int h = 0; h < 16; ++h) {
    const int rho = h & 3, cnt = rho == 0 ? NS : NN;
    for (int i = 0; i < cnt; ++i) { key[n] = ((2 * i + 1) * 100000 / (2 * cnt)) * 16 + h; val[n] = (h >> 2) | (rho << 2) | (i << 4); ++n; }
  }
  for (int a = 1; a < 256; ++a) { const int k = key[a], v = val[a]; int b = a - 1; while (b >= 0 && key[b] > k) { key[b + 1] = key[b]; val[b + 1] = val[b]; --b; } key[b + 1] = k; val[b + 1] = v; }
  Map m = {};
  for (int k = 0; k < 256; ++k) m.e[(k % 32) * 8 + k / 32] = (unsigned short)val[k];
  return m;
}

__device__ __forceinline__ unsigned cvtpk(float lo, float hi) {
  return __builtin_bit_cast(unsigned, __builtin_convertvector(f32x2{lo, hi}, bf16x2));
}
template <int CTRL> __device__ __forceinline__ float dpp(float x) {
  return __builtin_bit_cast(float, __builtin_amdgcn_mov_dpp(__builtin_bit_cast(int, x), CTRL, 0xf, 0xf, true));
}
__device__ __forceinline__ float xrow_max(float x) {
  auto s = __builtin_amdgcn_permlane16_swap(__float_as_uint(x), __float_as_uint(x), false, false);
  x = fmaxf(__uint_as_float(s[0]), __uint_as_float(s[1]));
  auto t = __builtin_amdgcn_permlane32_swap(__float_as_uint(x), __float_as_uint(x), false, false);
  return fmaxf(__uint_as_float(t[0]), __uint_as_float(t[1]));
}
__device__ __forceinline__ float xrow_sum(float x) {
  auto s = __builtin_amdgcn_permlane16_swap(__float_as_uint(x), __float_as_uint(x), false, false);
  x = __uint_as_float(s[0]) + __uint_as_float(s[1]);
  auto t = __builtin_amdgcn_permlane32_swap(__float_as_uint(x), __float_as_uint(x), false, false);
  return __uint_as_float(t[0]) + __uint_as_float(t[1]);
}
__device__ __forceinline__ float max3(float a, float b, float c) { float r; asm("v_max3_f32 %0, %1, %2, %3" : "=v"(r) : "v"(a), "v"(b), "v"(c)); return r; }
__device__ __forceinline__ void raw_barrier() { __builtin_amdgcn_sched_barrier(0); asm volatile("s_barrier" ::: "memory"); __builtin_amdgcn_sched_barrier(0); }
__device__ __forceinline__ void lds_barrier() { __builtin_amdgcn_sched_barrier(0); asm volatile("s_waitcnt lgkmcnt(0)\n\ts_barrier" ::: "memory"); __builtin_amdgcn_sched_barrier(0); }

#define STAMP(i)

__global__ void __launch_bounds__(W * 64, 2) attn_fused(Args) {
  __shared__ __attribute__((aligned(16))) float lds[L_END];
  const int bid = blockIdx.x;
  const CAS char* kap = (const CAS char*)__builtin_amdgcn_kernarg_segment_ptr();
  const u32x16 ka = *(const CAS u32x16*)kap;
  const unsigned ment2 = *(const CAS unsigned*)(kap + 64 + (bid >> 1) * 4);
  __builtin_amdgcn_sched_barrier(0);
  auto ptr64 = [&](int i) { return (unsigned long long)ka[2 * i] | ((unsigned long long)ka[2 * i + 1] << 32); };
  const int tid = threadIdx.x, lane = tid & 63, wave = __builtin_amdgcn_readfirstlane(tid >> 6);
  const int g = lane >> 4, c = lane & 15;

  const GAS int* bt = (const GAS int*)ptr64(3);
  const i32x4 bt0 = *reinterpret_cast<const GAS i32x4*>(bt + 4 * lane);
  const i32x4 bt1 = *reinterpret_cast<const GAS i32x4*>(bt + NB + 4 * lane);
  const CAS int* lens = (const CAS int*)ptr64(4);
  const int c0 = lens[0], c1 = lens[1];
  __builtin_amdgcn_sched_barrier(0);
  const unsigned ment = (bid & 1) ? ment2 >> 16 : ment2 & 0xffffu;
  const int quad = ment & 3, rho = (ment >> 2) & 3, s = ment >> 4;
  const int hs = (int)((1u - (ka[2] >> 8)) & 3u);
  const int kvh = 4 * quad + ((hs + rho) & 3);
  const bool foreign = rho == 0 && s >= 16;
  const int crho = foreign ? s - 15 : rho, crole = foreign ? 15 : s;
  const int chead = 4 * quad + ((hs + crho) & 3);
  const int r = c >> 2;
  const bf16x8 qf = *reinterpret_cast<const GAS bf16x8*>((const GAS char*)ptr64(0) + (kvh * 2 * D * 2) + (unsigned)(((r >> 1) * H + (r & 1)) * D * 2 + g * 64 + (c & 3) * 16));
  const __amdgpu_buffer_rsrc_t wrs = __builtin_amdgcn_make_buffer_rsrc((void*)ptr64(6), 0, WS_END, 0x00020000);
  const u32x2 e0 = __builtin_amdgcn_raw_buffer_load_b64(wrs, WS_E + kvh * 128, 0, AUX_SC1);
  const u32x2 e1 = __builtin_amdgcn_raw_buffer_load_b64(wrs, WS_E + chead * 128, 0, AUX_SC1);
  __builtin_amdgcn_sched_barrier(0);
  int* tl = reinterpret_cast<int*>(lds + L_TBL);
  tl[512] = (int)(OOB >> 16);
  __amdgpu_buffer_rsrc_t krs = __builtin_amdgcn_make_buffer_rsrc((void*)ptr64(1), 0, ka[14], 0x00020000);
  __amdgpu_buffer_rsrc_t vrs = __builtin_amdgcn_make_buffer_rsrc((void*)ptr64(2), 0, ka[14], 0x00020000);
  asm volatile("" : "+s"(krs), "+s"(vrs));
  constexpr unsigned RS = (1u << 20) / NS + 1, RN = (1u << 20) / NN + 1;
  unsigned R1 = rho == 0 ? RS : RN, sR = (unsigned)s * R1;
  int wv = wave;
  asm volatile("" : "+s"(sR), "+s"(R1), "+s"(wv));
  unsigned kvo = (c >> 2) * 4096 + g * 64 + (c & 3) * 16 + kvh * 256, vvo = g * 4096 + c * 16 + kvh * 256;
  asm volatile("" : "+v"(kvo), "+v"(vvo));
  float m = NEG, l = 0.f;
  f32x4 acc[8];
#pragma unroll
  for (int i = 0; i < 8; ++i) { acc[i] = f32x4{0.f, 0.f, 0.f, 0.f}; asm volatile("" : "+v"(acc[i])); }
  asm volatile("" : "+v"(m), "+v"(l));
  const unsigned lm0 = min((unsigned)(c0 - 1), (unsigned)(NB * PAGE - 1)), lm1 = min((unsigned)(c1 - 1), (unsigned)(NB * PAGE - 1));
  const int np0 = (int)(lm0 >> 4) + 1, np1 = (int)(lm1 >> 4) + 1;
  float cs = (r >> 1) ? (c1 <= 0 ? 0.f : C2) : (c0 <= 0 ? 0.f : C2);
  asm volatile("" : "+v"(cs));
  STAMP(1);
  __builtin_amdgcn_sched_barrier(0);
  *reinterpret_cast<i32x4*>(tl + 4 * lane) = bt0;
  *reinterpret_cast<i32x4*>(tl + NB + 4 * lane) = bt1;
  unsigned f4[4];
#pragma unroll
  for (int k = 0; k < 4; ++k) {
    const unsigned long long ne = __builtin_amdgcn_ballot_w64(bt0[k] != bt1[k]);
    asm("s_ff1_i32_b64 %0, %1\n\ts_lshl2_add_u32 %0, %0, %2" : "=s"(f4[k]) : "s"(ne), "i"(k) : "scc");
  }
  unsigned ps;
  asm("s_min_u32 %0, %1, %2\n\ts_min_u32 %0, %0, %3\n\ts_min_u32 %0, %0, %4\n\ts_min_u32 %0, %0, %5" : "=&s"(ps) : "s"(min((unsigned)np0, (unsigned)np1)), "s"(f4[0]), "s"(f4[1]), "s"(f4[2]), "s"(f4[3]) : "scc");
  const unsigned Q4 = 4u * (unsigned)(np0 + np1 - (int)ps);
  const int q0 = (int)((sR * Q4) >> 20), q1 = (int)min(((sR + R1) * Q4) >> 20, Q4);
  STAMP(2);
  int qb = q0;
  int qi, li, w1, nw; unsigned sov; bool b1;
  u32x4 K[NT][4], V[NT][4], Kx, Vx;
  auto fetchK = [&]() {
    const int n = min(q1 - qb, W * NI);
    const int w0 = qb + ((wv * n) >> 2);
    w1 = qb + (((wv + 1) * n) >> 2);
    nw = w1 - w0;
    qi = w0 + lane;
    const int ui = qi >> 2;
    b1 = ui >= np0;
    li = b1 ? ui + ((int)ps - np0) : ui;
    const int idx = qi < w1 ? (b1 ? li + NB : li) : 512;
    sov = ((unsigned)tl[idx] << 16) | ((unsigned)(qi & 3) << 14);
    STAMP(3);
#pragma unroll
    for (int t = 0; t < NT; ++t) {
#pragma unroll
      for (int k = 0; k < 4; ++k) K[t][k] = __builtin_amdgcn_raw_buffer_load_b128(krs, kvo, __builtin_amdgcn_readlane(sov, 4 * t + k), AUX_NT);
      __builtin_amdgcn_sched_barrier(0);
    }
    Kx = __builtin_amdgcn_raw_buffer_load_b128(krs, kvo, __builtin_amdgcn_readlane(sov, 4 * NT), AUX_NT);
    __builtin_amdgcn_sched_barrier(0);
  };
  fetchK();

  unsigned tpa, tpb, tca, tcb, pub_o, pub_ml, co0, co1, cm0, cm1, out_off;
  bool ho0, ho1, hm0, hm1;
  const int np = crho == 0 ? NS : NN;
  const bool bump = crole == (crho == 0 ? 0 : 15);

#pragma clang loop unroll(disable)
  for (;;) {
    const int tk = 16 * li + 4 * (qi & 3);
    const int nv0 = (qi < w1 && !b1) ? (int)lm0 + 1 - tk : 0;
    const int nv1 = (qi < w1 && (b1 || (qi >> 2) < (int)ps)) ? (int)lm1 + 1 - tk : 0;
    unsigned lm;
    {
      unsigned mb[2][4];
#pragma unroll
      for (int gg = 0; gg < 4; ++gg) { mb[0][gg] = (unsigned)__builtin_amdgcn_ballot_w64(nv0 > gg); mb[1][gg] = (unsigned)__builtin_amdgcn_ballot_w64(nv1 > gg); }
      const bool bb = r >> 1;
      const unsigned m0 = bb ? mb[1][0] : mb[0][0], m1 = bb ? mb[1][1] : mb[0][1], m2 = bb ? mb[1][2] : mb[0][2], m3 = bb ? mb[1][3] : mb[0][3];
      lm = (g & 2) ? ((g & 1) ? m3 : m2) : ((g & 1) ? m1 : m0);
    }
    unsigned mk[NT][4], mkx;
#pragma unroll
    for (int t = 0; t < NT; ++t)
#pragma unroll
      for (int k = 0; k < 4; ++k) { mk[t][k] = (unsigned)__builtin_amdgcn_sbfe((int)lm, 4 * t + k, 1); asm volatile("" : "+v"(mk[t][k])); }
    mkx = (unsigned)__builtin_amdgcn_sbfe((int)lm, 4 * NT, 1); asm volatile("" : "+v"(mkx));
    const int nwu = __builtin_amdgcn_readfirstlane(nw);
    raw_barrier();
    float sc[NT][4], scx = NEG;
    float mx = NEG;
    auto issueV = [&](int t) {
#pragma unroll
      for (int k = 0; k < 4; ++k) V[t][k] = __builtin_amdgcn_raw_buffer_load_b128(vrs, vvo, __builtin_amdgcn_readlane(sov, 4 * t + k), AUX_NT);
      __builtin_amdgcn_sched_barrier(0);
    };
    auto qk = [&](int t) {
      f32x4 d[4];
#pragma unroll
      for (int k = 0; k < 4; ++k) d[k] = __builtin_amdgcn_mfma_f32_16x16x32_bf16(__builtin_bit_cast(bf16x8, K[t][k]), qf, f32x4{0.f, 0.f, 0.f, 0.f}, 0, 0, 0);
      __builtin_amdgcn_sched_barrier(0);
      float y[4];
#pragma unroll
      for (int k = 0; k < 4; ++k) y[k] = dpp<0x00>(d[k][0]);
      __builtin_amdgcn_sched_barrier(0);
#pragma unroll
      for (int k = 0; k < 4; ++k) y[k] += dpp<0x55>(d[k][1]);
      __builtin_amdgcn_sched_barrier(0);
#pragma unroll
      for (int k = 0; k < 4; ++k) y[k] += dpp<0xAA>(d[k][2]);
      __builtin_amdgcn_sched_barrier(0);
#pragma unroll
      for (int k = 0; k < 4; ++k) y[k] += dpp<0xFF>(d[k][3]);
      __builtin_amdgcn_sched_barrier(0);
#pragma unroll
      for (int k = 0; k < 4; ++k) {
        const float z = y[k] * cs;
        sc[t][k] = __uint_as_float((__float_as_uint(z) & mk[t][k]) | (__float_as_uint(NEG) & ~mk[t][k]));
      }
      mx = max3(max3(mx, sc[t][0], sc[t][1]), sc[t][2], sc[t][3]);
      __builtin_amdgcn_sched_barrier(0);
    };
    issueV(0); qk(0); qk(1);
    issueV(1); qk(2); qk(3);
    issueV(2);
    if (nwu > 16) qk(4); else { sc[4][0] = sc[4][1] = sc[4][2] = sc[4][3] = NEG; }
    if (nwu > 20) {
      const f32x4 d = __builtin_amdgcn_mfma_f32_16x16x32_bf16(__builtin_bit_cast(bf16x8, Kx), qf, f32x4{0.f, 0.f, 0.f, 0.f}, 0, 0, 0);
      const float z = (dpp<0xFF>(d[3]) + (dpp<0xAA>(d[2]) + (dpp<0x55>(d[1]) + dpp<0x00>(d[0])))) * cs;
      scx = __uint_as_float((__float_as_uint(z) & mkx) | (__float_as_uint(NEG) & ~mkx));
      mx = fmaxf(mx, scx);
    }
    STAMP(4);
    mx = xrow_max(mx);
    if (g == 0 && (c & 3) == 0) lds[L_M + r * 4 + wave] = mx;
    lds_barrier();
    issueV(3);
    {
      const f32x4 mw = *reinterpret_cast<const f32x4*>(lds + L_M + r * 4);
      mx = fmaxf(fmaxf(fmaxf(mw[0], mw[1]), fmaxf(mw[2], mw[3])), m);
    }
    {
      const float alpha = __builtin_amdgcn_exp2f(m - mx);
      l *= alpha;
      const float ag = __shfl(alpha, 4 * g, 64);
#pragma unroll
      for (int i = 0; i < 8; ++i) acc[i][0] *= ag;
    }
    m = mx;
    u32x2 pa[NT];
#pragma unroll
    for (int t = 0; t < NT; ++t) {
      float p[4];
#pragma unroll
      for (int j = 0; j < 4; ++j) { p[j] = __builtin_amdgcn_exp2f(sc[t][j] - mx); l += p[j]; }
      pa[t] = u32x2{cvtpk(p[0], p[1]), cvtpk(p[2], p[3])};
      asm volatile("" : "+v"(pa[t]));
    }
    const float px = __builtin_amdgcn_exp2f(scx - mx);
    l += px;
    u32x2 pax = u32x2{cvtpk(px, 0.f), 0u};
    asm volatile("" : "+v"(pax));
    const float lsum = xrow_sum(l);
    if (g == 0 && (c & 3) == 0) lds[L_L + r * 4 + wave] = lsum;
    __builtin_amdgcn_sched_barrier(0);
    issueV(4);
    Vx = __builtin_amdgcn_raw_buffer_load_b128(vrs, vvo, __builtin_amdgcn_readlane(sov, 4 * NT), AUX_NT);
    __builtin_amdgcn_sched_barrier(0);
    {
      int z = 0;
      asm volatile("" : "+s"(z));
      tpa = __builtin_amdgcn_readfirstlane(e0[0]) + 1u + z; tpb = __builtin_amdgcn_readfirstlane(e0[1]) + 1u + z;
      tca = __builtin_amdgcn_readfirstlane(e1[0]) + 1u + z; tcb = __builtin_amdgcn_readfirstlane(e1[1]) + 1u + z;
      auto covers = [&](int a, int b, int bb) { const int t1 = bb == 0 ? 4 * np0 : 4 * (int)ps, t2 = bb == 0 ? 0x7fffffff : 4 * np0; return (a < b) & ((a < t1) | (b > t2)); };
      const int cz = c + z;
      const int pslot = (kvh * 16 + wave * 4 + g) * 32 + s + z;
      pub_o = covers(q0, q1, wave >> 1) ? WS_PO + pslot * 256 + cz * 16 : OOB;
      pub_ml = cz == 0 ? WS_PML + pslot * 16 : OOB;
      const unsigned RQ = (crho == 0 ? RS : RN) * Q4;
      const unsigned a0 = (unsigned)cz * RQ, a1 = a0 + 16u * RQ;
      ho0 = (cz < np) & covers((int)min(a0 >> 20, Q4), (int)min((a0 + RQ) >> 20, Q4), crole >> 3);
      ho1 = (cz + 16 < np) & covers((int)min(a1 >> 20, Q4), (int)min((a1 + RQ) >> 20, Q4), crole >> 3);
      hm0 = bump ? cz < np : ho0; hm1 = bump ? cz + 16 < np : ho1;
      const int cslot = (chead * 16 + crole) * 32 + cz;
      co0 = ho0 ? WS_PO + cslot * 256 + g * 64 : OOB; co1 = ho1 ? WS_PO + (cslot + 16) * 256 + g * 64 : OOB;
      cm0 = hm0 ? WS_PML + cslot * 16 : OOB; cm1 = hm1 ? WS_PML + (cslot + 16) * 16 : OOB;
      out_off = cz == 0 ? ((((crole >> 3) * H + chead * 2 + ((crole >> 2) & 1)) * D + (crole & 3) * 32 + g * 8) * 2) : OOB;
      asm volatile("" : "+s"(tpa), "+s"(tpb), "+s"(tca), "+s"(tcb), "+v"(pub_o), "+v"(pub_ml), "+v"(co0), "+v"(co1), "+v"(cm0), "+v"(cm1), "+v"(out_off));
      __builtin_amdgcn_sched_barrier(0);
    }
    STAMP(5);
    auto pv = [&](int t) {
      u32x2 bf[8];
#pragma unroll
      for (int e = 0; e < 4; ++e) {
        bf[2 * e] = u32x2{__builtin_amdgcn_perm(V[t][1][e], V[t][0][e], 0x05040100u), __builtin_amdgcn_perm(V[t][3][e], V[t][2][e], 0x05040100u)};
        bf[2 * e + 1] = u32x2{__builtin_amdgcn_perm(V[t][1][e], V[t][0][e], 0x07060302u), __builtin_amdgcn_perm(V[t][3][e], V[t][2][e], 0x07060302u)};
      }
      __builtin_amdgcn_sched_barrier(0);
#pragma unroll
      for (int i = 0; i < 8; ++i) acc[i] = __builtin_amdgcn_mfma_f32_16x16x16bf16_1k(__builtin_bit_cast(s16x4, pa[t]), __builtin_bit_cast(s16x4, bf[i]), acc[i], 0, 0, 0);
      __builtin_amdgcn_sched_barrier(0);
    };
    pv(0); pv(1); pv(2); pv(3);
    if (nwu > 16) pv(4);
    if (nwu > 20) {
#pragma unroll
      for (int e = 0; e < 4; ++e) {
        acc[2 * e] = __builtin_amdgcn_mfma_f32_16x16x16bf16_1k(__builtin_bit_cast(s16x4, pax), __builtin_bit_cast(s16x4, u32x2{Vx[e] & 0xffffu, 0u}), acc[2 * e], 0, 0, 0);
        acc[2 * e + 1] = __builtin_amdgcn_mfma_f32_16x16x16bf16_1k(__builtin_bit_cast(s16x4, pax), __builtin_bit_cast(s16x4, u32x2{Vx[e] >> 16, 0u}), acc[2 * e + 1], 0, 0, 0);
      }
    }
    qb += W * NI;
    if (qb >= q1) break;
    fetchK();
  }
  STAMP(6);
  {
    float* lo = lds + L_O + wave * 512 + g * 128 + c * 8;
    *reinterpret_cast<f32x4*>(lo) = f32x4{acc[0][0], acc[1][0], acc[2][0], acc[3][0]};
    *reinterpret_cast<f32x4*>(lo + 4) = f32x4{acc[4][0], acc[5][0], acc[6][0], acc[7][0]};
  }
  const float Mrow = __shfl(m, 4 * wave, 64);
  lds_barrier();
  STAMP(7);
  {
    f32x2 x[W];
#pragma unroll
    for (int w = 0; w < W; ++w) x[w] = *reinterpret_cast<const f32x2*>(lds + L_O + w * 512 + wave * 128 + 2 * lane);
    const f32x4 lw = *reinterpret_cast<const f32x4*>(lds + L_L + wave * 4);
    const f32x2 o = (x[0] + x[1]) + (x[2] + x[3]);
    __builtin_amdgcn_raw_buffer_store_b128(u32x4{__float_as_uint(o[0]), tpa, __float_as_uint(o[1]), tpb}, wrs, pub_o, 0, AUX_SC1);
    const float L = (lw[0] + lw[1]) + (lw[2] + lw[3]);
    __builtin_amdgcn_raw_buffer_store_b128(u32x4{__float_as_uint(Mrow), tpa, __float_as_uint(L), tpb}, wrs, pub_ml, 0, AUX_SC1);
    STAMP(8);
  }
  if (wave == CONS_WAVE) {
    u32x4 x0[4], x1[4], ml0, ml1;
    const bool two = crho == 0;
    __builtin_amdgcn_s_sleep(12);
#pragma unroll
    for (int k = 0; k < 4; ++k) x0[k] = __builtin_amdgcn_raw_buffer_load_b128(wrs, co0 + k * 16, 0, AUX_SC1);
    ml0 = __builtin_amdgcn_raw_buffer_load_b128(wrs, cm0, 0, AUX_SC1);
    if (two) {
#pragma unroll
      for (int k = 0; k < 4; ++k) x1[k] = __builtin_amdgcn_raw_buffer_load_b128(wrs, co1 + k * 16, 0, AUX_SC1);
      ml1 = __builtin_amdgcn_raw_buffer_load_b128(wrs, cm1, 0, AUX_SC1);
    } else {
#pragma unroll
      for (int k = 0; k < 4; ++k) x1[k] = u32x4{0u, 0u, 0u, 0u};
      ml1 = u32x4{0u, 0u, 0u, 0u};
    }
#pragma clang loop unroll(disable)
    for (unsigned spins = 0; spins < (1u << 20); ++spins) {
      unsigned d0 = 0, d1 = 0;
#pragma unroll
      for (int k = 0; k < 4; ++k) { d0 |= (x0[k][1] ^ tca) | (x0[k][3] ^ tcb); d1 |= (x1[k][1] ^ tca) | (x1[k][3] ^ tcb); }
      const bool po0 = ho0 && d0 != 0u, po1 = ho1 && d1 != 0u;
      const bool pm0 = hm0 && ((ml0[1] ^ tca) | (ml0[3] ^ tcb)) != 0u, pm1 = hm1 && ((ml1[1] ^ tca) | (ml1[3] ^ tcb)) != 0u;
      if (!__any(po0 || po1 || pm0 || pm1)) break;
      if (po0) {
#pragma unroll
        for (int k = 0; k < 4; ++k) x0[k] = __builtin_amdgcn_raw_buffer_load_b128(wrs, co0 + k * 16, 0, AUX_SC1);
      }
      if (pm0) ml0 = __builtin_amdgcn_raw_buffer_load_b128(wrs, cm0, 0, AUX_SC1);
      if (po1) {
#pragma unroll
        for (int k = 0; k < 4; ++k) x1[k] = __builtin_amdgcn_raw_buffer_load_b128(wrs, co1 + k * 16, 0, AUX_SC1);
      }
      if (pm1) ml1 = __builtin_amdgcn_raw_buffer_load_b128(wrs, cm1, 0, AUX_SC1);
      asm volatile("" ::: "memory");
    }
    STAMP(9);
    const float mp0 = hm0 ? __uint_as_float(ml0[0]) : NEG, mp1 = hm1 ? __uint_as_float(ml1[0]) : NEG;
    float M = fmaxf(mp0, mp1);
    asm("s_nop 1\n\tv_max_f32_dpp %0, %0, %0 row_ror:8 row_mask:0xf bank_mask:0xf\n\ts_nop 1\n\tv_max_f32_dpp %0, %0, %0 row_ror:4 row_mask:0xf bank_mask:0xf\n\t"
        "s_nop 1\n\tv_max_f32_dpp %0, %0, %0 row_ror:2 row_mask:0xf bank_mask:0xf\n\ts_nop 1\n\tv_max_f32_dpp %0, %0, %0 row_ror:1 row_mask:0xf bank_mask:0xf" : "+v"(M));
    const float f0 = __builtin_amdgcn_exp2f(mp0 - M), f1 = __builtin_amdgcn_exp2f(mp1 - M);
    float L = fmaf(__uint_as_float(ml1[2]), f1, __uint_as_float(ml0[2]) * f0);
    float o8[8];
#pragma unroll
    for (int k = 0; k < 8; ++k) o8[k] = fmaf(__uint_as_float(x1[k >> 1][(k & 1) * 2]), f1, __uint_as_float(x0[k >> 1][(k & 1) * 2]) * f0);
#define RS9(R) "v_add_f32_dpp %0, %0, %0 row_ror:" R " row_mask:0xf bank_mask:0xf\n\tv_add_f32_dpp %1, %1, %1 row_ror:" R " row_mask:0xf bank_mask:0xf\n\t" \
               "v_add_f32_dpp %2, %2, %2 row_ror:" R " row_mask:0xf bank_mask:0xf\n\tv_add_f32_dpp %3, %3, %3 row_ror:" R " row_mask:0xf bank_mask:0xf\n\t" \
               "v_add_f32_dpp %4, %4, %4 row_ror:" R " row_mask:0xf bank_mask:0xf\n\tv_add_f32_dpp %5, %5, %5 row_ror:" R " row_mask:0xf bank_mask:0xf\n\t" \
               "v_add_f32_dpp %6, %6, %6 row_ror:" R " row_mask:0xf bank_mask:0xf\n\tv_add_f32_dpp %7, %7, %7 row_ror:" R " row_mask:0xf bank_mask:0xf\n\t" \
               "v_add_f32_dpp %8, %8, %8 row_ror:" R " row_mask:0xf bank_mask:0xf\n\t"
    asm("s_nop 1\n\t" RS9("8") RS9("4") RS9("2") RS9("1")
        : "+v"(L), "+v"(o8[0]), "+v"(o8[1]), "+v"(o8[2]), "+v"(o8[3]), "+v"(o8[4]), "+v"(o8[5]), "+v"(o8[6]), "+v"(o8[7]));
#undef RS9
    const float inv = __builtin_amdgcn_rcpf(L);
    const u32x4 pk = {cvtpk(o8[0] * inv, o8[1] * inv), cvtpk(o8[2] * inv, o8[3] * inv), cvtpk(o8[4] * inv, o8[5] * inv), cvtpk(o8[6] * inv, o8[7] * inv)};
    const __amdgpu_buffer_rsrc_t ors = __builtin_amdgcn_make_buffer_rsrc((void*)ptr64(5), 0, 2 * H * D * 2, 0x00020000);
    __builtin_amdgcn_raw_buffer_store_b128(pk, ors, out_off, 0, 0);
    if (bump && lane == 0) __builtin_amdgcn_raw_buffer_store_b64(u32x2{tca, tcb}, wrs, WS_E + chead * 128, 0, AUX_SC1);
    STAMP(10);
  }
}

extern "C" void kernel_launch(void* const* d_in, const int* in_sizes, int n_in, void* d_out, int out_size, void* d_ws, size_t ws_size, hipStream_t stream) {
  static constexpr Map kMap = make_map();
  Args a;
  a.q = d_in[0]; a.k = d_in[1]; a.v = d_in[2]; a.bt = (const int*)d_in[3]; a.lens = (const int*)d_in[4];
  a.out = d_out; a.ws = d_ws; a.kv_bytes = (unsigned)in_sizes[1] * 2u; a.pad = 0; a.map = kMap;
  attn_fused<<<256, W * 64, 0, stream>>>(a);
}
```

```cpp
#include <hip/hip_runtime.h>
#include <stdint.h>

typedef __bf16 bf16x8 __attribute__((ext_vector_type(8)));
typedef short s16x4 __attribute__((ext_vector_type(4)));
typedef float f32x4 __attribute__((ext_vector_type(4)));
typedef float f32x2 __attribute__((ext_vector_type(2)));
typedef __bf16 bf16x2 __attribute__((ext_vector_type(2)));
typedef unsigned u32x4 __attribute__((ext_vector_type(4)));
typedef unsigned u32x2 __attribute__((ext_vector_type(2)));
typedef int i32x4 __attribute__((ext_vector_type(4)));
typedef unsigned u32x16 __attribute__((ext_vector_type(16)));
#define GAS __attribute__((address_space(1)))
#define CAS __attribute__((address_space(4)))

constexpr int H = 32, D = 128, PAGE = 16, NB = 256;
constexpr int AUX_NT = 2, AUX_SC1 = 16;
constexpr unsigned OOB = 0x80000000u;
constexpr int NS = 19, NN = 15;
static_assert(NS + 3 * NN == 64 && NS - 16 == 3 && NN == 15, "consumer roles below");
constexpr int W = 4, NT = 5, NI = 4 * NT + 1;
constexpr int CONS_WAVE = 3;
constexpr float NEG = -1e30f;
constexpr float C2 = 0.088388347648318440f * 1.4426950408889634f;
constexpr unsigned WS_E = 0, WS_PO = 4096, WS_PML = WS_PO + 16 * 16 * 32 * 256, WS_END = WS_PML + 16 * 16 * 32 * 16;
constexpr int L_O = 0, L_M = W * 512, L_L = L_M + 16, L_TBL = L_L + 16, L_END = L_TBL + 516;

struct Map { unsigned short e[256]; };
struct Args { const void* q; const void* k; const void* v; const int* bt; const int* lens; void* out; void* ws; unsigned kv_bytes; unsigned pad; Map map; };

constexpr Map make_map() {
  int key[256] = {}, val[256] = {};
  int n = 0;
  for (int h = 0; h < 16; ++h) {
    const int rho = h & 3, cnt = rho == 0 ? NS : NN;
    for (int i = 0; i < cnt; ++i) { key[n] = ((2 * i + 1) * 100000 / (2 * cnt)) * 16 + h; val[n] = (h >> 2) | (rho << 2) | (i << 4); ++n; }
  }
  for (int a = 1; a < 256; ++a) { const int k = key[a], v = val[a]; int b = a - 1; while (b >= 0 && key[b] > k) { key[b + 1] = key[b]; val[b + 1] = val[b]; --b; } key[b + 1] = k; val[b + 1] = v; }
  Map m = {};
  for (int k = 0; k < 256; ++k) m.e[(k % 32) * 8 + k / 32] = (unsigned short)val[k];
  return m;
}

__device__ __forceinline__ unsigned cvtpk(float lo, float hi) {
  return __builtin_bit_cast(unsigned, __builtin_convertvector(f32x2{lo, hi}, bf16x2));
}
template <int CTRL> __device__ __forceinline__ float dpp(float x) {
  return __builtin_bit_cast(float, __builtin_amdgcn_mov_dpp(__builtin_bit_cast(int, x), CTRL, 0xf, 0xf, true));
}
__device__ __forceinline__ float xrow_max(float x) {
  auto s = __builtin_amdgcn_permlane16_swap(__float_as_uint(x), __float_as_uint(x), false, false);
  x = fmaxf(__uint_as_float(s[0]), __uint_as_float(s[1]));
  auto t = __builtin_amdgcn_permlane32_swap(__float_as_uint(x), __float_as_uint(x), false, false);
  return fmaxf(__uint_as_float(t[0]), __uint_as_float(t[1]));
}
__device__ __forceinline__ float xrow_sum(float x) {
  auto s = __builtin_amdgcn_permlane16_swap(__float_as_uint(x), __float_as_uint(x), false, false);
  x = __uint_as_float(s[0]) + __uint_as_float(s[1]);
  auto t = __builtin_amdgcn_permlane32_swap(__float_as_uint(x), __float_as_uint(x), false, false);
  return __uint_as_float(t[0]) + __uint_as_float(t[1]);
}
__device__ __forceinline__ float max3(float a, float b, float c) { float r; asm("v_max3_f32 %0, %1, %2, %3" : "=v"(r) : "v"(a), "v"(b), "v"(c)); return r; }
__device__ __forceinline__ void raw_barrier() { __builtin_amdgcn_sched_barrier(0); asm volatile("s_barrier" ::: "memory"); __builtin_amdgcn_sched_barrier(0); }
__device__ __forceinline__ void lds_barrier() { __builtin_amdgcn_sched_barrier(0); asm volatile("s_waitcnt lgkmcnt(0)\n\ts_barrier" ::: "memory"); __builtin_amdgcn_sched_barrier(0); }

__global__ void __launch_bounds__(W * 64, 2) attn_fused(Args) {
  __shared__ __attribute__((aligned(16))) float lds[L_END];
  const int bid = blockIdx.x;
  const CAS char* kap = (const CAS char*)__builtin_amdgcn_kernarg_segment_ptr();
  const u32x16 ka = *(const CAS u32x16*)kap;
  const unsigned ment2 = *(const CAS unsigned*)(kap + 64 + (bid >> 1) * 4);
  __builtin_amdgcn_sched_barrier(0);
  auto ptr64 = [&](int i) { return (unsigned long long)ka[2 * i] | ((unsigned long long)ka[2 * i + 1] << 32); };
  const int tid = threadIdx.x, lane = tid & 63, wave = __builtin_amdgcn_readfirstlane(tid >> 6);
  const int g = lane >> 4, c = lane & 15;

  const GAS int* bt = (const GAS int*)ptr64(3);
  const i32x4 bt0 = *reinterpret_cast<const GAS i32x4*>(bt + 4 * lane);
  const i32x4 bt1 = *reinterpret_cast<const GAS i32x4*>(bt + NB + 4 * lane);
  const CAS int* lens = (const CAS int*)ptr64(4);
  const int c0 = lens[0], c1 = lens[1];
  __builtin_amdgcn_sched_barrier(0);
  const unsigned ment = (bid & 1) ? ment2 >> 16 : ment2 & 0xffffu;
  const int quad = ment & 3, rho = (ment >> 2) & 3, s = ment >> 4;
  const int hs = (int)((1u - (ka[2] >> 8)) & 3u);
  const int kvh = 4 * quad + ((hs + rho) & 3);
  const bool foreign = rho == 0 && s >= 16;
  const int crho = foreign ? s - 15 : rho, crole = foreign ? 15 : s;
  const int chead = 4 * quad + ((hs + crho) & 3);
  const int r = c >> 2;
  const bf16x8 qf = *reinterpret_cast<const GAS bf16x8*>((const GAS char*)ptr64(0) + (kvh * 2 * D * 2) + (unsigned)(((r >> 1) * H + (r & 1)) * D * 2 + g * 64 + (c & 3) * 16));
  const __amdgpu_buffer_rsrc_t wrs = __builtin_amdgcn_make_buffer_rsrc((void*)ptr64(6), 0, WS_END, 0x00020000);
  const u32x2 e0 = __builtin_amdgcn_raw_buffer_load_b64(wrs, WS_E + kvh * 128, 0, AUX_SC1);
  const u32x2 e1 = __builtin_amdgcn_raw_buffer_load_b64(wrs, WS_E + chead * 128, 0, AUX_SC1);
  __builtin_amdgcn_sched_barrier(0);
  int* tl = reinterpret_cast<int*>(lds + L_TBL);
  tl[512] = (int)(OOB >> 16);
  __amdgpu_buffer_rsrc_t krs = __builtin_amdgcn_make_buffer_rsrc((void*)ptr64(1), 0, ka[14], 0x00020000);
  __amdgpu_buffer_rsrc_t vrs = __builtin_amdgcn_make_buffer_rsrc((void*)ptr64(2), 0, ka[14], 0x00020000);
  asm volatile("" : "+s"(krs), "+s"(vrs));
  constexpr unsigned RS = (1u << 20) / NS + 1, RN = (1u << 20) / NN + 1;
  unsigned R1 = rho == 0 ? RS : RN, sR = (unsigned)s * R1;
  int wv = wave;
  asm volatile("" : "+s"(sR), "+s"(R1), "+s"(wv));
  unsigned kvo = (c >> 2) * 4096 + g * 64 + (c & 3) * 16 + kvh * 256, vvo = g * 4096 + c * 16 + kvh * 256;
  asm volatile("" : "+v"(kvo), "+v"(vvo));
  float m = NEG, l = 0.f;
  f32x4 acc[8];
#pragma unroll
  for (int i = 0; i < 8; ++i) { acc[i] = f32x4{0.f, 0.f, 0.f, 0.f}; asm volatile("" : "+v"(acc[i])); }
  asm volatile("" : "+v"(m), "+v"(l));
  const unsigned lm0 = min((unsigned)(c0 - 1), (unsigned)(NB * PAGE - 1)), lm1 = min((unsigned)(c1 - 1), (unsigned)(NB * PAGE - 1));
  const int np0 = (int)(lm0 >> 4) + 1, np1 = (int)(lm1 >> 4) + 1;
  float cs = (r >> 1) ? (c1 <= 0 ? 0.f : C2) : (c0 <= 0 ? 0.f : C2);
  asm volatile("" : "+v"(cs));
  __builtin_amdgcn_sched_barrier(0);
  *reinterpret_cast<i32x4*>(tl + 4 * lane) = bt0;
  *reinterpret_cast<i32x4*>(tl + NB + 4 * lane) = bt1;
  unsigned f4[4];
#pragma unroll
  for (int k = 0; k < 4; ++k) {
    const unsigned long long ne = __builtin_amdgcn_ballot_w64(bt0[k] != bt1[k]);
    asm("s_ff1_i32_b64 %0, %1\n\ts_lshl2_add_u32 %0, %0, %2" : "=s"(f4[k]) : "s"(ne), "i"(k) : "scc");
  }
  unsigned ps;
  asm("s_min_u32 %0, %1, %2\n\ts_min_u32 %0, %0, %3\n\ts_min_u32 %0, %0, %4\n\ts_min_u32 %0, %0, %5" : "=&s"(ps) : "s"(min((unsigned)np0, (unsigned)np1)), "s"(f4[0]), "s"(f4[1]), "s"(f4[2]), "s"(f4[3]) : "scc");
  const unsigned Q4 = 4u * (unsigned)(np0 + np1 - (int)ps);
  const int q0 = (int)((sR * Q4) >> 20), q1 = (int)min(((sR + R1) * Q4) >> 20, Q4);
  int qb = q0;
  int qi, li, w1, nw; unsigned sov; bool b1;
  u32x4 K[NT][4], V[NT][4], Kx, Vx;
  auto fetchK = [&]() {
    w1 = min(q1, qb + W * NI);
    nw = (w1 - qb - wv + 3) >> 2;
    qi = qb + wv + 4 * lane;
    const int ui = qi >> 2;
    b1 = ui >= np0;
    li = b1 ? ui + ((int)ps - np0) : ui;
    const int idx = qi < w1 ? (b1 ? li + NB : li) : 512;
    sov = ((unsigned)tl[idx] << 16) | ((unsigned)(qi & 3) << 14);
#pragma unroll
    for (int t = 0; t < NT; ++t) {
#pragma unroll
      for (int k = 0; k < 4; ++k) K[t][k] = __builtin_amdgcn_raw_buffer_load_b128(krs, kvo, __builtin_amdgcn_readlane(sov, 4 * t + k), AUX_NT);
      __builtin_amdgcn_sched_barrier(0);
    }
    Kx = __builtin_amdgcn_raw_buffer_load_b128(krs, kvo, __builtin_amdgcn_readlane(sov, 4 * NT), AUX_NT);
    __builtin_amdgcn_sched_barrier(0);
  };
  fetchK();

  unsigned tpa, tpb, tca, tcb, pub_o, pub_ml, co0, co1, cm0, cm1, out_off;
  bool ho0, ho1, hm0, hm1;
  const int np = crho == 0 ? NS : NN;
  const bool bump = crole == (crho == 0 ? 0 : 15);

#pragma clang loop unroll(disable)
  for (;;) {
    const int tk = 16 * li + 4 * (qi & 3);
    const int nv0 = (qi < w1 && !b1) ? (int)lm0 + 1 - tk : 0;
    const int nv1 = (qi < w1 && (b1 || (qi >> 2) < (int)ps)) ? (int)lm1 + 1 - tk : 0;
    unsigned lm;
    {
      unsigned mb[2][4];
#pragma unroll
      for (int gg = 0; gg < 4; ++gg) { mb[0][gg] = (unsigned)__builtin_amdgcn_ballot_w64(nv0 > gg); mb[1][gg] = (unsigned)__builtin_amdgcn_ballot_w64(nv1 > gg); }
      const bool bb = r >> 1;
      const unsigned m0 = bb ? mb[1][0] : mb[0][0], m1 = bb ? mb[1][1] : mb[0][1], m2 = bb ? mb[1][2] : mb[0][2], m3 = bb ? mb[1][3] : mb[0][3];
      lm = (g & 2) ? ((g & 1) ? m3 : m2) : ((g & 1) ? m1 : m0);
    }
    unsigned mk[NT][4], mkx;
#pragma unroll
    for (int t = 0; t < NT; ++t)
#pragma unroll
      for (int k = 0; k < 4; ++k) { mk[t][k] = (unsigned)__builtin_amdgcn_sbfe((int)lm, 4 * t + k, 1); asm volatile("" : "+v"(mk[t][k])); }
    mkx = (unsigned)__builtin_amdgcn_sbfe((int)lm, 4 * NT, 1); asm volatile("" : "+v"(mkx));
    const int nwu = __builtin_amdgcn_readfirstlane(nw);
    raw_barrier();
    float sc[NT][4], scx = NEG;
    float mx = NEG;
    auto issueV = [&](int t) {
#pragma unroll
      for (int k = 0; k < 4; ++k) V[t][k] = __builtin_amdgcn_raw_buffer_load_b128(vrs, vvo, __builtin_amdgcn_readlane(sov, 4 * t + k), AUX_NT);
      __builtin_amdgcn_sched_barrier(0);
    };
    auto qk = [&](int t) {
      f32x4 d[4];
#pragma unroll
      for (int k = 0; k < 4; ++k) d[k] = __builtin_amdgcn_mfma_f32_16x16x32_bf16(__builtin_bit_cast(bf16x8, K[t][k]), qf, f32x4{0.f, 0.f, 0.f, 0.f}, 0, 0, 0);
      __builtin_amdgcn_sched_barrier(0);
      float y[4];
#pragma unroll
      for (int k = 0; k < 4; ++k) y[k] = dpp<0x00>(d[k][0]);
      __builtin_amdgcn_sched_barrier(0);
#pragma unroll
      for (int k = 0; k < 4; ++k) y[k] += dpp<0x55>(d[k][1]);
      __builtin_amdgcn_sched_barrier(0);
#pragma unroll
      for (int k = 0; k < 4; ++k) y[k] += dpp<0xAA>(d[k][2]);
      __builtin_amdgcn_sched_barrier(0);
#pragma unroll
      for (int k = 0; k < 4; ++k) y[k] += dpp<0xFF>(d[k][3]);
      __builtin_amdgcn_sched_barrier(0);
#pragma unroll
      for (int k = 0; k < 4; ++k) {
        const float z = y[k] * cs;
        sc[t][k] = __uint_as_float((__float_as_uint(z) & mk[t][k]) | (__float_as_uint(NEG) & ~mk[t][k]));
      }
      mx = max3(max3(mx, sc[t][0], sc[t][1]), sc[t][2], sc[t][3]);
      __builtin_amdgcn_sched_barrier(0);
    };
    issueV(0); qk(0); qk(1);
    issueV(1); qk(2); qk(3);
    issueV(2);
    if (nwu > 16) qk(4); else { sc[4][0] = sc[4][1] = sc[4][2] = sc[4][3] = NEG; }
    if (nwu > 20) {
      const f32x4 d = __builtin_amdgcn_mfma_f32_16x16x32_bf16(__builtin_bit_cast(bf16x8, Kx), qf, f32x4{0.f, 0.f, 0.f, 0.f}, 0, 0, 0);
      const float z = (dpp<0xFF>(d[3]) + (dpp<0xAA>(d[2]) + (dpp<0x55>(d[1]) + dpp<0x00>(d[0])))) * cs;
      scx = __uint_as_float((__float_as_uint(z) & mkx) | (__float_as_uint(NEG) & ~mkx));
      mx = fmaxf(mx, scx);
    }
    mx = xrow_max(mx);
    if (g == 0 && (c & 3) == 0) lds[L_M + r * 4 + wave] = mx;
    lds_barrier();
    issueV(3);
    {
      const f32x4 mw = *reinterpret_cast<const f32x4*>(lds + L_M + r * 4);
      mx = fmaxf(fmaxf(fmaxf(mw[0], mw[1]), fmaxf(mw[2], mw[3])), m);
    }
    {
      const float alpha = __builtin_amdgcn_exp2f(m - mx);
      l *= alpha;
      const float ag = __shfl(alpha, 4 * g, 64);
#pragma unroll
      for (int i = 0; i < 8; ++i) acc[i][0] *= ag;
    }
    m = mx;
    u32x2 pa[NT];
#pragma unroll
    for (int t = 0; t < NT; ++t) {
      float p[4];
#pragma unroll
      for (int j = 0; j < 4; ++j) { p[j] = __builtin_amdgcn_exp2f(sc[t][j] - mx); l += p[j]; }
      pa[t] = u32x2{cvtpk(p[0], p[1]), cvtpk(p[2], p[3])};
      asm volatile("" : "+v"(pa[t]));
    }
    const float px = __builtin_amdgcn_exp2f(scx - mx);
    l += px;
    u32x2 pax = u32x2{cvtpk(px, 0.f), 0u};
    asm volatile("" : "+v"(pax));
    const float lsum = xrow_sum(l);
    if (g == 0 && (c & 3) == 0) lds[L_L + r * 4 + wave] = lsum;
    __builtin_amdgcn_sched_barrier(0);
    issueV(4);
    Vx = __builtin_amdgcn_raw_buffer_load_b128(vrs, vvo, __builtin_amdgcn_readlane(sov, 4 * NT), AUX_NT);
    __builtin_amdgcn_sched_barrier(0);
    {
      int z = 0;
      asm volatile("" : "+s"(z));
      tpa = __builtin_amdgcn_readfirstlane(e0[0]) + 1u + z; tpb = __builtin_amdgcn_readfirstlane(e0[1]) + 1u + z;
      tca = __builtin_amdgcn_readfirstlane(e1[0]) + 1u + z; tcb = __builtin_amdgcn_readfirstlane(e1[1]) + 1u + z;
      auto covers = [&](int a, int b, int bb) { const int t1 = bb == 0 ? 4 * np0 : 4 * (int)ps, t2 = bb == 0 ? 0x7fffffff : 4 * np0; return (a < b) & ((a < t1) | (b > t2)); };
      const int cz = c + z;
      const int pslot = (kvh * 16 + wave * 4 + g) * 32 + s + z;
      pub_o = covers(q0, q1, wave >> 1) ? WS_PO + pslot * 256 + cz * 16 : OOB;
      pub_ml = cz == 0 ? WS_PML + pslot * 16 : OOB;
      const unsigned RQ = (crho == 0 ? RS : RN) * Q4;
      const unsigned a0 = (unsigned)cz * RQ, a1 = a0 + 16u * RQ;
      ho0 = (cz < np) & covers((int)min(a0 >> 20, Q4), (int)min((a0 + RQ) >> 20, Q4), crole >> 3);
      ho1 = (cz + 16 < np) & covers((int)min(a1 >> 20, Q4), (int)min((a1 + RQ) >> 20, Q4), crole >> 3);
      hm0 = bump ? cz < np : ho0; hm1 = bump ? cz + 16 < np : ho1;
      const int cslot = (chead * 16 + crole) * 32 + cz;
      co0 = ho0 ? WS_PO + cslot * 256 + g * 64 : OOB; co1 = ho1 ? WS_PO + (cslot + 16) * 256 + g * 64 : OOB;
      cm0 = hm0 ? WS_PML + cslot * 16 : OOB; cm1 = hm1 ? WS_PML + (cslot + 16) * 16 : OOB;
      out_off = cz == 0 ? ((((crole >> 3) * H + chead * 2 + ((crole >> 2) & 1)) * D + (crole & 3) * 32 + g * 8) * 2) : OOB;
      asm volatile("" : "+s"(tpa), "+s"(tpb), "+s"(tca), "+s"(tcb), "+v"(pub_o), "+v"(pub_ml), "+v"(co0), "+v"(co1), "+v"(cm0), "+v"(cm1), "+v"(out_off));
      __builtin_amdgcn_sched_barrier(0);
    }
    auto pv = [&](int t) {
      u32x2 bf[8];
#pragma unroll
      for (int e = 0; e < 4; ++e) {
        bf[2 * e] = u32x2{__builtin_amdgcn_perm(V[t][1][e], V[t][0][e], 0x05040100u), __builtin_amdgcn_perm(V[t][3][e], V[t][2][e], 0x05040100u)};
        bf[2 * e + 1] = u32x2{__builtin_amdgcn_perm(V[t][1][e], V[t][0][e], 0x07060302u), __builtin_amdgcn_perm(V[t][3][e], V[t][2][e], 0x07060302u)};
      }
      __builtin_amdgcn_sched_barrier(0);
#pragma unroll
      for (int i = 0; i < 8; ++i) acc[i] = __builtin_amdgcn_mfma_f32_16x16x16bf16_1k(__builtin_bit_cast(s16x4, pa[t]), __builtin_bit_cast(s16x4, bf[i]), acc[i], 0, 0, 0);
      __builtin_amdgcn_sched_barrier(0);
    };
    pv(0); pv(1); pv(2); pv(3);
    if (nwu > 16) pv(4);
    if (nwu > 20) {
#pragma unroll
      for (int e = 0; e < 4; ++e) {
        acc[2 * e] = __builtin_amdgcn_mfma_f32_16x16x16bf16_1k(__builtin_bit_cast(s16x4, pax), __builtin_bit_cast(s16x4, u32x2{Vx[e] & 0xffffu, 0u}), acc[2 * e], 0, 0, 0);
        acc[2 * e + 1] = __builtin_amdgcn_mfma_f32_16x16x16bf16_1k(__builtin_bit_cast(s16x4, pax), __builtin_bit_cast(s16x4, u32x2{Vx[e] >> 16, 0u}), acc[2 * e + 1], 0, 0, 0);
      }
    }
    qb += W * NI;
    if (qb >= q1) break;
    fetchK();
  }
  {
    float* lo = lds + L_O + wave * 512 + g * 128 + c * 8;
    *reinterpret_cast<f32x4*>(lo) = f32x4{acc[0][0], acc[1][0], acc[2][0], acc[3][0]};
    *reinterpret_cast<f32x4*>(lo + 4) = f32x4{acc[4][0], acc[5][0], acc[6][0], acc[7][0]};
  }
  const float Mrow = __shfl(m, 4 * wave, 64);
  lds_barrier();
  {
    f32x2 x[W];
#pragma unroll
    for (int w = 0; w < W; ++w) x[w] = *reinterpret_cast<const f32x2*>(lds + L_O + w * 512 + wave * 128 + 2 * lane);
    const f32x4 lw = *reinterpret_cast<const f32x4*>(lds + L_L + wave * 4);
    const f32x2 o = (x[0] + x[1]) + (x[2] + x[3]);
    __builtin_amdgcn_raw_buffer_store_b128(u32x4{__float_as_uint(o[0]), tpa, __float_as_uint(o[1]), tpb}, wrs, pub_o, 0, AUX_SC1);
    const float L = (lw[0] + lw[1]) + (lw[2] + lw[3]);
    __builtin_amdgcn_raw_buffer_store_b128(u32x4{__float_as_uint(Mrow), tpa, __float_as_uint(L), tpb}, wrs, pub_ml, 0, AUX_SC1);
  }
  if (wave == CONS_WAVE) {
    u32x4 x0[4], x1[4], ml0, ml1;
    const bool two = crho == 0;
    __builtin_amdgcn_s_sleep(12);
#pragma unroll
    for (int k = 0; k < 4; ++k) x0[k] = __builtin_amdgcn_raw_buffer_load_b128(wrs, co0 + k * 16, 0, AUX_SC1);
    ml0 = __builtin_amdgcn_raw_buffer_load_b128(wrs, cm0, 0, AUX_SC1);
    if (two) {
#pragma unroll
      for (int k = 0; k < 4; ++k) x1[k] = __builtin_amdgcn_raw_buffer_load_b128(wrs, co1 + k * 16, 0, AUX_SC1);
      ml1 = __builtin_amdgcn_raw_buffer_load_b128(wrs, cm1, 0, AUX_SC1);
    } else {
#pragma unroll
      for (int k = 0; k < 4; ++k) x1[k] = u32x4{0u, 0u, 0u, 0u};
      ml1 = u32x4{0u, 0u, 0u, 0u};
    }
#pragma clang loop unroll(disable)
    for (unsigned spins = 0; spins < (1u << 20); ++spins) {
      unsigned d0 = 0, d1 = 0;
#pragma unroll
      for (int k = 0; k < 4; ++k) { d0 |= (x0[k][1] ^ tca) | (x0[k][3] ^ tcb); d1 |= (x1[k][1] ^ tca) | (x1[k][3] ^ tcb); }
      const bool po0 = ho0 && d0 != 0u, po1 = ho1 && d1 != 0u;
      const bool pm0 = hm0 && ((ml0[1] ^ tca) | (ml0[3] ^ tcb)) != 0u, pm1 = hm1 && ((ml1[1] ^ tca) | (ml1[3] ^ tcb)) != 0u;
      if (!__any(po0 || po1 || pm0 || pm1)) break;
      if (po0) {
#pragma unroll
        for (int k = 0; k < 4; ++k) x0[k] = __builtin_amdgcn_raw_buffer_load_b128(wrs, co0 + k * 16, 0, AUX_SC1);
      }
      if (pm0) ml0 = __builtin_amdgcn_raw_buffer_load_b128(wrs, cm0, 0, AUX_SC1);
      if (po1) {
#pragma unroll
        for (int k = 0; k < 4; ++k) x1[k] = __builtin_amdgcn_raw_buffer_load_b128(wrs, co1 + k * 16, 0, AUX_SC1);
      }
      if (pm1) ml1 = __builtin_amdgcn_raw_buffer_load_b128(wrs, cm1, 0, AUX_SC1);
      asm volatile("" ::: "memory");
    }
    const float mp0 = hm0 ? __uint_as_float(ml0[0]) : NEG, mp1 = hm1 ? __uint_as_float(ml1[0]) : NEG;
    float M = fmaxf(mp0, mp1);
    asm("s_nop 1\n\tv_max_f32_dpp %0, %0, %0 row_ror:8 row_mask:0xf bank_mask:0xf\n\ts_nop 1\n\tv_max_f32_dpp %0, %0, %0 row_ror:4 row_mask:0xf bank_mask:0xf\n\t"
        "s_nop 1\n\tv_max_f32_dpp %0, %0, %0 row_ror:2 row_mask:0xf bank_mask:0xf\n\ts_nop 1\n\tv_max_f32_dpp %0, %0, %0 row_ror:1 row_mask:0xf bank_mask:0xf" : "+v"(M));
    const float f0 = __builtin_amdgcn_exp2f(mp0 - M), f1 = __builtin_amdgcn_exp2f(mp1 - M);
    float L = fmaf(__uint_as_float(ml1[2]), f1, __uint_as_float(ml0[2]) * f0);
    float o8[8];
#pragma unroll
    for (int k = 0; k < 8; ++k) o8[k] = fmaf(__uint_as_float(x1[k >> 1][(k & 1) * 2]), f1, __uint_as_float(x0[k >> 1][(k & 1) * 2]) * f0);
#define RS9(R) "v_add_f32_dpp %0, %0, %0 row_ror:" R " row_mask:0xf bank_mask:0xf\n\tv_add_f32_dpp %1, %1, %1 row_ror:" R " row_mask:0xf bank_mask:0xf\n\t" \
               "v_add_f32_dpp %2, %2, %2 row_ror:" R " row_mask:0xf bank_mask:0xf\n\tv_add_f32_dpp %3, %3, %3 row_ror:" R " row_mask:0xf bank_mask:0xf\n\t" \
               "v_add_f32_dpp %4, %4, %4 row_ror:" R " row_mask:0xf bank_mask:0xf\n\tv_add_f32_dpp %5, %5, %5 row_ror:" R " row_mask:0xf bank_mask:0xf\n\t" \
               "v_add_f32_dpp %6, %6, %6 row_ror:" R " row_mask:0xf bank_mask:0xf\n\tv_add_f32_dpp %7, %7, %7 row_ror:" R " row_mask:0xf bank_mask:0xf\n\t" \
               "v_add_f32_dpp %8, %8, %8 row_ror:" R " row_mask:0xf bank_mask:0xf\n\t"
    asm("s_nop 1\n\t" RS9("8") RS9("4") RS9("2") RS9("1")
        : "+v"(L), "+v"(o8[0]), "+v"(o8[1]), "+v"(o8[2]), "+v"(o8[3]), "+v"(o8[4]), "+v"(o8[5]), "+v"(o8[6]), "+v"(o8[7]));
#undef RS9
    const float inv = __builtin_amdgcn_rcpf(L);
    const u32x4 pk = {cvtpk(o8[0] * inv, o8[1] * inv), cvtpk(o8[2] * inv, o8[3] * inv), cvtpk(o8[4] * inv, o8[5] * inv), cvtpk(o8[6] * inv, o8[7] * inv)};
    const __amdgpu_buffer_rsrc_t ors = __builtin_amdgcn_make_buffer_rsrc((void*)ptr64(5), 0, 2 * H * D * 2, 0x00020000);
    __builtin_amdgcn_raw_buffer_store_b128(pk, ors, out_off, 0, 0);
    if (bump && lane == 0) __builtin_amdgcn_raw_buffer_store_b64(u32x2{tca, tcb}, wrs, WS_E + chead * 128, 0, AUX_SC1);
  }
}

extern "C" void kernel_launch(void* const* d_in, const int* in_sizes, int n_in, void* d_out, int out_size, void* d_ws, size_t ws_size, hipStream_t stream) {
  static constexpr Map kMap = make_map();
  Args a;
  a.q = d_in[0]; a.k = d_in[1]; a.v = d_in[2]; a.bt = (const int*)d_in[3]; a.lens = (const int*)d_in[4];
  a.out = d_out; a.ws = d_ws; a.kv_bytes = (unsigned)in_sizes[1] * 2u; a.pad = 0; a.map = kMap;
  attn_fused<<<256, W * 64, 0, stream>>>(a);
}
```
